```python
import torch

def _softmax(x, axis=-1):
    return torch.softmax(x, dim=axis)
def _gelu(x):
    return torch.nn.functional.gelu(x, approximate="tanh")
def _silu(x):
    return torch.nn.functional.silu(x)
def _sigmoid(x):
    return torch.sigmoid(x)
def _layernorm(x, axis=-1, eps=1e-5):
    m = x.mean(dim=axis, keepdim=True)
    v = x.var(dim=axis, keepdim=True, unbiased=False)
    return (x - m) / torch.sqrt(v + eps)
def _scatter_add(tgt, idx, src, axis=0):
    return tgt.clone().index_add_(axis, idx, src)
def _segment_sum(src, seg, n):
    out = torch.zeros((n,) + src.shape[1:], dtype=src.dtype)
    return out.index_add_(0, seg, src)
def _pad(x, pw):
    flat = tuple(v for pair in reversed(pw) for v in pair)
    return torch.nn.functional.pad(x, flat)

kernel_name = 'standalone__mha_block_cached__near_ridge'

def setup_inputs():

    _w = (lambda a: (lambda t: t.to(torch.bfloat16) if t.is_floating_point() else t)(torch.as_tensor(a)))
    B, Sq, D = 8, 128, 640
    H, Sk, Dh = 32, 1024, 128
    return {
        "x": _w((lambda *s: torch.randn(*s, dtype=torch.bfloat16) * 0.8)(B, Sq, D)),
        "k_cache": _w((lambda *s: torch.randn(*s, dtype=torch.bfloat16) * 0.8)(B, H, Sk, Dh)),
        "v_cache": _w((lambda *s: torch.randn(*s, dtype=torch.bfloat16) * 0.05)(B, H, Sk, Dh)),
        "wq": _w((lambda *s: torch.randn(*s, dtype=torch.bfloat16) * 0.8)(D, H * Dh) * (D ** -0.5)),
        "wo": _w((lambda *s: torch.randn(*s, dtype=torch.bfloat16) * 0.05)(H * Dh, D) * ((H * Dh) ** -0.5)),
    }

def reference(x, k_cache, v_cache, wq, wo):
    B, Sq, D = x.shape
    H, Dh = 32, 128
    q = (lambda x, *d: x.permute(*d))((x @ wq).reshape(B, Sq, H, Dh), 0, 2, 1, 3)
    scores = torch.einsum("bhqd,bhkd->bhqk", q, k_cache) * (Dh ** -0.5)
    p = _softmax(scores, axis=-1)
    o = torch.einsum("bhqk,bhkd->bhqd", p, v_cache)
    return (lambda x, *d: x.permute(*d))(o, 0, 2, 1, 3).reshape(B, Sq, H * Dh) @ wo
```

```cpp
#include <hip/hip_runtime.h>
#include <hip/hip_bf16.h>
#include <cstdio>
#include <cstdint>

typedef float    f32x16 __attribute__((ext_vector_type(16)));
typedef float    f32x4  __attribute__((ext_vector_type(4)));
typedef float    f32x2  __attribute__((ext_vector_type(2)));
typedef __bf16   bf16x8 __attribute__((ext_vector_type(8)));
typedef __bf16   bf16x2 __attribute__((ext_vector_type(2)));
typedef short    s16x4  __attribute__((ext_vector_type(4)));
typedef short    s16x8  __attribute__((ext_vector_type(8)));
typedef unsigned u32x4  __attribute__((ext_vector_type(4)));
typedef unsigned u32x2  __attribute__((ext_vector_type(2)));
#define DI __device__ __forceinline__
#define LAS __attribute__((address_space(3)))
typedef LAS const char* lds_cptr;
typedef LAS char* lds_ptr;

constexpr int NB = 8, SQ = 128, DM = 640, NH = 32, SK = 1024, DH = 128;
constexpr int HD = NH * DH;
constexpr float SCL2E = 0.08838834764831845f * 1.4426950408889634f;

#define MFMA32(a, b, c) __builtin_amdgcn_mfma_f32_32x32x16_bf16((a), (b), (c), 0, 0, 0)
#define SBAR() __builtin_amdgcn_sched_barrier(0)
#define WAIT_BAR(N) asm volatile("s_waitcnt vmcnt(" #N ") lgkmcnt(0)\n\ts_barrier" ::: "memory")
#define BAR_LGKM() asm volatile("s_waitcnt lgkmcnt(0)\n\ts_barrier" ::: "memory")

DI unsigned rfl(unsigned x) { return __builtin_amdgcn_readfirstlane(x); }
DI unsigned cvtpk(float lo, float hi) { f32x2 v = {lo, hi}; bf16x2 b = __builtin_convertvector(v, bf16x2); return __builtin_bit_cast(unsigned, b); }

template <bool NT>
DI void glds16(const void* sbase, unsigned voff, unsigned lds_base) {
  unsigned sv;
  if constexpr (NT)
    asm volatile("s_mov_b32 %0, m0\n\ts_mov_b32 m0, %3\n\ts_nop 0\n\tglobal_load_lds_dwordx4 %1, %2 nt\n\ts_mov_b32 m0, %0"
                 : "=&s"(sv) : "v"(voff), "s"(sbase), "s"(lds_base) : "memory");
  else
    asm volatile("s_mov_b32 %0, m0\n\ts_mov_b32 m0, %3\n\ts_nop 0\n\tglobal_load_lds_dwordx4 %1, %2\n\ts_mov_b32 m0, %0"
                 : "=&s"(sv) : "v"(voff), "s"(sbase), "s"(lds_base) : "memory");
}
DI bf16x8 lds_rd128(lds_cptr p) { return *(const LAS bf16x8*)p; }
typedef short v4i16_t __attribute__((ext_vector_type(4)));
DI s16x4 lds_tr(lds_cptr p) { return __builtin_bit_cast(s16x4, __builtin_amdgcn_ds_read_tr16_b64_v4i16((LAS v4i16_t*)p)); }
DI bf16x8 cat8(s16x4 lo, s16x4 hi) { s16x8 v = __builtin_shufflevector(lo, hi, 0, 1, 2, 3, 4, 5, 6, 7); return __builtin_bit_cast(bf16x8, v); }
DI bf16x8 pack8(const f32x16& x, int s) {
  u32x4 p = {cvtpk(x[8 * s], x[8 * s + 1]), cvtpk(x[8 * s + 2], x[8 * s + 3]), cvtpk(x[8 * s + 4], x[8 * s + 5]), cvtpk(x[8 * s + 6], x[8 * s + 7])};
  return __builtin_bit_cast(bf16x8, p);
}
#define MX3(a, b, c) __builtin_fmaxf(__builtin_fmaxf((a), (b)), (c))
DI float xhalf_max(float m) { auto rr = __builtin_amdgcn_permlane32_swap(__float_as_uint(m), __float_as_uint(m), false, false); return __builtin_fmaxf(__uint_as_float(rr[0]), __uint_as_float(rr[1])); }
DI float xhalf_sum(float m) { auto rr = __builtin_amdgcn_permlane32_swap(__float_as_uint(m), __float_as_uint(m), false, false); return __uint_as_float(rr[0]) + __uint_as_float(rr[1]); }

#define KV_NT true

constexpr unsigned PAIRB = 32768, SLOTB = 16384;
constexpr int LDS_BYTES = 163840;

DI void bh_of(int bid, int& b, int& h) { const int x = bid & 7, j = bid >> 3; b = j >> 2; h = 4 * x + (j & 3); }

__global__ __launch_bounds__(512, 2) void attn_fused(const __hip_bfloat16* __restrict__ xin, const __hip_bfloat16* __restrict__ kc,
                                                     const __hip_bfloat16* __restrict__ vc, const __hip_bfloat16* __restrict__ wq,
                                                     u32x4* __restrict__ obuf) {
  extern __shared__ __attribute__((aligned(16))) char lds_raw[];
  const lds_ptr lds = (lds_ptr)lds_raw;
  const unsigned lds0 = rfl((unsigned)(uintptr_t)lds_raw);
  const int tid = threadIdx.x, lane = tid & 63, r32 = lane & 31, hi = lane >> 5;
  const int wid = rfl(tid >> 6), qi = wid & 3, grp = wid >> 2;
  int b, h; bh_of(blockIdx.x, b, h);

  const int lr = (lane >> 2) & 7, lb = (lane >> 4) & 1;
  const char* kbase = (const char*)kc + (size_t)(b * NH + h) * SK * DH * 2;
  const char* vbase = (const char*)vc + (size_t)(b * NH + h) * SK * DH * 2;
  const char* xbase = (const char*)xin + (size_t)b * SQ * DM * 2;
  const char* wbase = (const char*)wq + (size_t)h * DH * 2;
  unsigned voffA[4], voffB[4];
  if (grp) {
#pragma unroll
    for (int ii = 0; ii < 4; ++ii) { const int row = 16 * qi + 8 * (ii >> 1) + lr, xr = 2 * (ii >> 1) + lb, ch = 8 * (ii & 1) + 4 * hi + ((lane & 3) ^ xr);
      voffA[ii] = row * 256 + ch * 16; voffB[ii] = voffA[ii]; }
  } else {
#pragma unroll
    for (int ii = 0; ii < 4; ++ii) {
      { const int row = 32 * qi + 8 * ii + lr, xr = (2 * ii + lb) & 3, ch = 4 * hi + ((lane & 3) ^ xr);
        voffA[ii] = row * (DM * 2) + ch * 16; }
      { const int row = 16 * qi + 8 * (ii >> 1) + lr, xr = 2 * (ii >> 1) + lb, ch = 8 * (ii & 1) + 4 * hi + ((lane & 3) ^ xr);
        voffB[ii] = row * (HD * 2) + ch * 16; }
    }
  }
  const unsigned dq = lds0 + qi * 4096;
  auto issue_q = [&](int s, unsigned pslot) {
    const char* xs = xbase + s * 128; const char* ws = wbase + (size_t)s * 64 * HD * 2;
#pragma unroll
    for (int ii = 0; ii < 4; ++ii) glds16<false>(xs, voffA[ii], dq + pslot + ii * 1024);
#pragma unroll
    for (int ii = 0; ii < 4; ++ii) glds16<false>(ws, voffB[ii], dq + pslot + SLOTB + ii * 1024);
  };
  auto issue_kv = [&](int u, unsigned pslot) {
    if (u < 16) { const char* ks = kbase + u * SLOTB;
#pragma unroll
      for (int ii = 0; ii < 4; ++ii) glds16<KV_NT>(ks, voffA[ii], dq + pslot + ii * 1024); }
    if (u > 0) { const char* vs = vbase + (u - 1) * SLOTB;
#pragma unroll
      for (int ii = 0; ii < 4; ++ii) glds16<KV_NT>(vs, voffA[ii], dq + pslot + SLOTB + ii * 1024); }
  };

  if (!grp) { issue_q(0, 0); issue_q(1, PAIRB); issue_q(2, 2 * PAIRB); }
  else { issue_kv(0, 3 * PAIRB); issue_kv(1, 4 * PAIRB); }

  const unsigned xr_row = (r32 >> 2) & 3;
  const unsigned krd0 = 2048 * (r32 >> 3) + 64 * (r32 & 7) + 16 * ((0 + hi) ^ xr_row);
  const unsigned krd1 = 2048 * (r32 >> 3) + 64 * (r32 & 7) + 16 * ((2 + hi) ^ xr_row);
  const unsigned xrd0 = 4096 * qi + 1024 * (r32 >> 3) + 64 * (r32 & 7) + 16 * ((0 + hi) ^ xr_row);
  const unsigned xrd1 = 4096 * qi + 1024 * (r32 >> 3) + 64 * (r32 & 7) + 16 * ((2 + hi) ^ xr_row);
  const int tq = (lane & 15) >> 2, tp = lane & 3, blk = (lane >> 4) & 1;
  const int pch = 2 * blk + (tp & 1), phalf = tp >> 1;
  const unsigned vrd0 = 64 * (4 * hi + tq) + 16 * (pch ^ (0 + hi)) + 8 * phalf;
  const unsigned vrd1 = 2048 + 64 * (4 * hi + tq) + 16 * (pch ^ (2 + hi)) + 8 * phalf;
  const unsigned wrd0 = 2048 * hi + 64 * (0 + tq) + 16 * (pch ^ (2 * hi + 0)) + 8 * phalf;
  const unsigned wrd1 = 2048 * hi + 64 * (4 + tq) + 16 * (pch ^ (2 * hi + 1)) + 8 * phalf;

  f32x16 qa0 = {}, qa1 = {};
  {
    unsigned ps = 0;
#pragma unroll 1
    for (int s = 0; s < 10; ++s) {
      if (!grp) { if (s == 0) { WAIT_BAR(16); } else if (s < 9) { WAIT_BAR(8); } else { WAIT_BAR(0); } }
      else { BAR_LGKM(); }
      const unsigned pprev = ps == 0 ? 2 * PAIRB : ps - PAIRB;
      if (!grp) { if (s >= 1 && s + 2 <= 9) issue_q(s + 2, pprev); }
      else { if (s >= 8) issue_kv(s - 6, pprev); }
      const lds_cptr xs = (lds_cptr)lds + ps, ws = (lds_cptr)lds + ps + SLOTB + grp * 1024;
#pragma unroll
      for (int ks = 0; ks < 4; ++ks) {
        const bf16x8 xb = lds_rd128(xs + ((ks & 1) ? xrd1 : xrd0) + 512 * (ks >> 1));
        const bf16x8 a0 = cat8(lds_tr(ws + wrd0 + 4096 * ks), lds_tr(ws + wrd1 + 4096 * ks));
        const bf16x8 a1 = cat8(lds_tr(ws + wrd0 + 4096 * ks + 512), lds_tr(ws + wrd1 + 4096 * ks + 512));
        qa0 = MFMA32(a0, xb, qa0);
        qa1 = MFMA32(a1, xb, qa1);
      }
      ps = ps == 2 * PAIRB ? 0 : ps + PAIRB;
    }
  }
  bf16x8 qf[8];
  {
    const bf16x8 o0 = pack8(qa0, 0), o1 = pack8(qa0, 1), o2 = pack8(qa1, 0), o3 = pack8(qa1, 1);
    BAR_LGKM();
    const lds_ptr mine = lds + wid * 4096 + lane * 16; const lds_cptr theirs = (lds_cptr)lds + (wid ^ 4) * 4096 + lane * 16;
    *(LAS bf16x8*)(mine) = o0; *(LAS bf16x8*)(mine + 1024) = o1; *(LAS bf16x8*)(mine + 2048) = o2; *(LAS bf16x8*)(mine + 3072) = o3;
    BAR_LGKM();
    const bf16x8 t0 = lds_rd128(theirs), t1 = lds_rd128(theirs + 1024), t2 = lds_rd128(theirs + 2048), t3 = lds_rd128(theirs + 3072);
    if (!grp) { qf[0] = o0; qf[1] = o1; qf[2] = o2; qf[3] = o3; qf[4] = t0; qf[5] = t1; qf[6] = t2; qf[7] = t3; }
    else      { qf[0] = t0; qf[1] = t1; qf[2] = t2; qf[3] = t3; qf[4] = o0; qf[5] = o1; qf[6] = o2; qf[7] = o3; }
  }

  f32x16 o[4] = {};
  float mrun = -__builtin_inff(), lrun = 0.f;
  bf16x8 pf[4];
  auto slot_of = [](int u) -> unsigned { const int m = u % 5; return (m == 0 ? 3u : m == 1 ? 4u : m == 2 ? 1u : m == 3 ? 2u : 0u) * PAIRB; };
#pragma unroll 1
  for (int p = 0; p <= 16; ++p) {
    if (grp) {
      if (p <= 12) { WAIT_BAR(24); } else if (p == 13) { WAIT_BAR(20); } else if (p == 14) { WAIT_BAR(12); } else if (p == 15) { WAIT_BAR(4); } else { WAIT_BAR(0); }
      if (p + 4 <= 16) issue_kv(p + 4, slot_of(p + 4));
    } else { BAR_LGKM(); }
    const unsigned ps = slot_of(p);
    if ((p & 1) == grp) {
      if (p < 16) {
        const lds_cptr ks_ = (lds_cptr)lds + ps;
        f32x16 s0 = {}, s1 = {};
#pragma unroll
        for (int s = 0; s < 8; ++s) {
          const bf16x8 k0 = lds_rd128(ks_ + ((s & 1) ? krd1 : krd0) + 512 * (s >> 1));
          const bf16x8 k1 = lds_rd128(ks_ + ((s & 1) ? krd1 : krd0) + 512 * (s >> 1) + 8192);
          s0 = MFMA32(k0, qf[s], s0);
          s1 = MFMA32(k1, qf[s], s1);
        }
        float ma = MX3(s0[0], s0[1], s1[0]), mb = MX3(s0[2], s0[3], s1[1]); ma = MX3(ma, s1[2], s1[3]);
#pragma unroll
        for (int r = 4; r < 16; r += 4) { ma = MX3(ma, s0[r], s0[r + 1]); mb = MX3(mb, s0[r + 2], s0[r + 3]); ma = MX3(ma, s1[r], s1[r + 1]); mb = MX3(mb, s1[r + 2], s1[r + 3]); }
        const float mt = xhalf_max(__builtin_fmaxf(ma, mb));
        const float mnew = __builtin_fmaxf(mrun, mt);
        const float alpha = __builtin_amdgcn_exp2f((mrun - mnew) * SCL2E);
        const float nm = -mnew * SCL2E;
        float sum = 0.f;
#pragma unroll
        for (int r = 0; r < 16; ++r) { s0[r] = __builtin_amdgcn_exp2f(__builtin_fmaf(s0[r], SCL2E, nm)); sum += s0[r]; }
#pragma unroll
        for (int r = 0; r < 16; ++r) { s1[r] = __builtin_amdgcn_exp2f(__builtin_fmaf(s1[r], SCL2E, nm)); sum += s1[r]; }
        lrun = lrun * alpha + sum;
        if (__any(mnew > mrun)) {
#pragma unroll
          for (int d = 0; d < 4; ++d)
#pragma unroll
            for (int r = 0; r < 16; ++r) o[d][r] *= alpha;
        }
        mrun = mnew;
        pf[0] = pack8(s0, 0); pf[1] = pack8(s0, 1); pf[2] = pack8(s1, 0); pf[3] = pack8(s1, 1);
      }
    } else {
      if (p >= 1) {
        const lds_cptr vs_ = (lds_cptr)lds + ps + SLOTB;
#pragma unroll
        for (int kk = 0; kk < 4; ++kk) {
#pragma unroll
          for (int d = 0; d < 4; ++d) {
            const bf16x8 a = cat8(lds_tr(vs_ + vrd0 + 4096 * kk + 512 * d), lds_tr(vs_ + vrd1 + 4096 * kk + 512 * d));
            o[d] = MFMA32(a, pf[kk], o[d]);
          }
        }
      }
    }
  }
  lrun = xhalf_sum(lrun);
  {
    const lds_ptr mine = lds + wid * 8192 + lane * 16; const lds_cptr theirs = (lds_cptr)lds + (wid ^ 4) * 8192 + lane * 16;
    const f32x16& g0 = grp ? o[0] : o[2]; const f32x16& g1 = grp ? o[1] : o[3];
#pragma unroll
    for (int i = 0; i < 4; ++i) {
      *(LAS f32x4*)(mine + i * 1024) = (f32x4){g0[4 * i], g0[4 * i + 1], g0[4 * i + 2], g0[4 * i + 3]};
      *(LAS f32x4*)(mine + 4096 + i * 1024) = (f32x4){g1[4 * i], g1[4 * i + 1], g1[4 * i + 2], g1[4 * i + 3]};
    }
    *(LAS f32x2*)(lds + 65536 + wid * 512 + lane * 8) = (f32x2){mrun, lrun};
    BAR_LGKM();
    const f32x2 st = *(const LAS f32x2*)(lds + 65536 + (wid ^ 4) * 512 + lane * 8);
    const float mo = st[0], lo = st[1];
    const float m = __builtin_fmaxf(mrun, mo);
    const float fa = __builtin_amdgcn_exp2f((mrun - m) * SCL2E), fb = __builtin_amdgcn_exp2f((mo - m) * SCL2E);
    const float inv = 1.f / (fa * lrun + fb * lo);
    const float ca = fa * inv, cb = fb * inv;
    f32x16 k0 = grp ? o[2] : o[0], k1 = grp ? o[3] : o[1];
#pragma unroll
    for (int i = 0; i < 4; ++i) {
      const f32x4 t0 = *(const LAS f32x4*)(theirs + i * 1024), t1 = *(const LAS f32x4*)(theirs + 4096 + i * 1024);
#pragma unroll
      for (int e = 0; e < 4; ++e) { k0[4 * i + e] = k0[4 * i + e] * ca + t0[e] * cb; k1[4 * i + e] = k1[4 * i + e] * ca + t1[e] * cb; }
    }
    u32x4* dst = obuf + ((size_t)((b * NH + h) * 4 + qi) * 8 + 4 * grp) * 64 + lane;
    dst[0] = __builtin_bit_cast(u32x4, pack8(k0, 0)); dst[64] = __builtin_bit_cast(u32x4, pack8(k0, 1));
    dst[128] = __builtin_bit_cast(u32x4, pack8(k1, 0)); dst[192] = __builtin_bit_cast(u32x4, pack8(k1, 1));
  }
}

__global__ __launch_bounds__(512, 2) void oproj_partial(const u32x4* __restrict__ obuf, const __hip_bfloat16* __restrict__ wo, float* __restrict__ part) {
  extern __shared__ __attribute__((aligned(16))) char lds_raw[];
  const lds_ptr lds = (lds_ptr)lds_raw;
  const unsigned lds0 = rfl((unsigned)(uintptr_t)lds_raw);
  const int tid = threadIdx.x, lane = tid & 63, r32 = lane & 31, hi = lane >> 5;
  const int wid = rfl(tid >> 6), qi = wid & 3, kh = wid >> 2;
  int b, h; bh_of(blockIdx.x, b, h);
  const int g = h >> 2, r = h & 3;
  const char* wbase = (const char*)wo + ((size_t)512 * g * DM + 160 * r) * 2;
#pragma unroll 4
  for (int i = 0; i < 20; ++i) {
    const int piece = wid * 20 + i, c = piece * 64 + lane, row = c / 20, ch = c - row * 20;
    glds16<false>(wbase, row * (DM * 2) + ch * 16, lds0 + piece * 1024);
  }
  bf16x8 of[16];
#pragma unroll
  for (int i = 0; i < 16; ++i) { const int hh = 4 * g + 2 * kh + (i >> 3);
    of[i] = __builtin_bit_cast(bf16x8, obuf[((size_t)((b * NH + hh) * 4 + qi) * 8 + (i & 7)) * 64 + lane]); }
  WAIT_BAR(0);
  const int tq = (lane & 15) >> 2, tp = lane & 3, blk = (lane >> 4) & 1;
  const lds_cptr wr = (lds_cptr)lds + (256 * kh + 8 * hi + tq) * 320 + 32 * blk + 8 * tp;
  f32x16 acc[5] = {};
#pragma unroll
  for (int i = 0; i < 16; ++i) {
#pragma unroll
    for (int nt = 0; nt < 5; ++nt) {
      const bf16x8 a = cat8(lds_tr(wr + i * 16 * 320 + nt * 64), lds_tr(wr + (i * 16 + 4) * 320 + nt * 64));
      acc[nt] = MFMA32(a, of[i], acc[nt]);
    }
  }
  float* dst = part + ((size_t)(b * 16 + 2 * g + kh) * SQ + 32 * qi + r32) * DM + 160 * r + 4 * hi;
#pragma unroll
  for (int nt = 0; nt < 5; ++nt)
#pragma unroll
    for (int i = 0; i < 4; ++i)
      *(f32x4*)(dst + 32 * nt + 8 * i) = (f32x4){acc[nt][4 * i], acc[nt][4 * i + 1], acc[nt][4 * i + 2], acc[nt][4 * i + 3]};
}

__global__ __launch_bounds__(256) void oproj_reduce(const float* __restrict__ part, u32x2* __restrict__ out) {
  const int i = blockIdx.x * 256 + threadIdx.x;
  const int b = i / (SQ * DM / 4), rem = i - b * (SQ * DM / 4);
  const f32x4* p = (const f32x4*)part + (size_t)b * 16 * (SQ * DM / 4) + rem;
  f32x4 s = p[0];
#pragma unroll
  for (int k = 1; k < 16; ++k) s += p[(size_t)k * (SQ * DM / 4)];
  out[i] = (u32x2){cvtpk(s[0], s[1]), cvtpk(s[2], s[3])};
}

extern "C" void kernel_launch(void* const* d_in, const int* in_sizes, int n_in, void* d_out, int out_size, void* d_ws, size_t ws_size, hipStream_t stream) {
  const __hip_bfloat16* x = (const __hip_bfloat16*)d_in[0];
  const __hip_bfloat16* kc = (const __hip_bfloat16*)d_in[1];
  const __hip_bfloat16* vc = (const __hip_bfloat16*)d_in[2];
  const __hip_bfloat16* wq = (const __hip_bfloat16*)d_in[3];
  const __hip_bfloat16* wo = (const __hip_bfloat16*)d_in[4];
  u32x4* obuf = (u32x4*)d_ws;
  float* part = (float*)((char*)d_ws + (size_t)NB * NH * SQ * DH * 2);
  (void)hipFuncSetAttribute((const void*)attn_fused, hipFuncAttributeMaxDynamicSharedMemorySize, LDS_BYTES);
  (void)hipFuncSetAttribute((const void*)oproj_partial, hipFuncAttributeMaxDynamicSharedMemorySize, LDS_BYTES);
  hipLaunchKernelGGL(attn_fused, dim3(256), dim3(512), LDS_BYTES, stream, x, kc, vc, wq, obuf);
  hipLaunchKernelGGL(oproj_partial, dim3(256), dim3(512), LDS_BYTES, stream, (const u32x4*)obuf, wo, part);
  hipLaunchKernelGGL(oproj_reduce, dim3(NB * SQ * DM / 4 / 256), dim3(256), 0, stream, (const float*)part, (u32x2*)d_out);
}
```

```cpp
#include <hip/hip_runtime.h>
#include <hip/hip_bf16.h>
#include <cstdio>
#include <cstdint>

typedef float    f32x16 __attribute__((ext_vector_type(16)));
typedef float    f32x4  __attribute__((ext_vector_type(4)));
typedef float    f32x2  __attribute__((ext_vector_type(2)));
typedef __bf16   bf16x8 __attribute__((ext_vector_type(8)));
typedef __bf16   bf16x2 __attribute__((ext_vector_type(2)));
typedef short    s16x4  __attribute__((ext_vector_type(4)));
typedef short    s16x8  __attribute__((ext_vector_type(8)));
typedef unsigned u32x4  __attribute__((ext_vector_type(4)));
typedef unsigned u32x2  __attribute__((ext_vector_type(2)));
#define DI __device__ __forceinline__
#define LAS __attribute__((address_space(3)))
typedef LAS const char* lds_cptr;
typedef LAS char* lds_ptr;

constexpr int NB = 8, SQ = 128, DM = 640, NH = 32, SK = 1024, DH = 128;
constexpr int HD = NH * DH;
constexpr float SCL2E = 0.08838834764831845f * 1.4426950408889634f;

#define MFMA32(a, b, c) __builtin_amdgcn_mfma_f32_32x32x16_bf16((a), (b), (c), 0, 0, 0)
#define SBAR() __builtin_amdgcn_sched_barrier(0)
#define WAIT_BAR(N) asm volatile("s_waitcnt vmcnt(" #N ") lgkmcnt(0)\n\ts_barrier" ::: "memory")
#define BAR_LGKM() asm volatile("s_waitcnt lgkmcnt(0)\n\ts_barrier" ::: "memory")

DI unsigned rfl(unsigned x) { return __builtin_amdgcn_readfirstlane(x); }
DI unsigned cvtpk(float lo, float hi) { f32x2 v = {lo, hi}; bf16x2 b = __builtin_convertvector(v, bf16x2); return __builtin_bit_cast(unsigned, b); }

template <bool NT>
DI void glds16(const void* sbase, unsigned voff, unsigned lds_base) {
  unsigned sv;
  if constexpr (NT)
    asm volatile("s_mov_b32 %0, m0\n\ts_mov_b32 m0, %3\n\ts_nop 0\n\tglobal_load_lds_dwordx4 %1, %2 nt\n\ts_mov_b32 m0, %0"
                 : "=&s"(sv) : "v"(voff), "s"(sbase), "s"(lds_base) : "memory");
  else
    asm volatile("s_mov_b32 %0, m0\n\ts_mov_b32 m0, %3\n\ts_nop 0\n\tglobal_load_lds_dwordx4 %1, %2\n\ts_mov_b32 m0, %0"
                 : "=&s"(sv) : "v"(voff), "s"(sbase), "s"(lds_base) : "memory");
}
DI bf16x8 lds_rd128(lds_cptr p) { return *(const LAS bf16x8*)p; }
typedef short v4i16_t __attribute__((ext_vector_type(4)));
DI s16x4 lds_tr(lds_cptr p) { return __builtin_bit_cast(s16x4, __builtin_amdgcn_ds_read_tr16_b64_v4i16((LAS v4i16_t*)p)); }
DI bf16x8 cat8(s16x4 lo, s16x4 hi) { s16x8 v = __builtin_shufflevector(lo, hi, 0, 1, 2, 3, 4, 5, 6, 7); return __builtin_bit_cast(bf16x8, v); }
DI bf16x8 pack8(const f32x16& x, int s) {
  u32x4 p = {cvtpk(x[8 * s], x[8 * s + 1]), cvtpk(x[8 * s + 2], x[8 * s + 3]), cvtpk(x[8 * s + 4], x[8 * s + 5]), cvtpk(x[8 * s + 6], x[8 * s + 7])};
  return __builtin_bit_cast(bf16x8, p);
}
#define MX3(a, b, c) __builtin_fmaxf(__builtin_fmaxf((a), (b)), (c))
DI float xhalf_max(float m) { auto rr = __builtin_amdgcn_permlane32_swap(__float_as_uint(m), __float_as_uint(m), false, false); return __builtin_fmaxf(__uint_as_float(rr[0]), __uint_as_float(rr[1])); }
DI float xhalf_sum(float m) { auto rr = __builtin_amdgcn_permlane32_swap(__float_as_uint(m), __float_as_uint(m), false, false); return __uint_as_float(rr[0]) + __uint_as_float(rr[1]); }

#define STAMP_DECL
#define STAMP(i)
#define RSTAMP(i)
#define STAMP_FLUSH()
#define KV_NT true

constexpr unsigned PAIRB = 32768, SLOTB = 16384;
constexpr int LDS_BYTES = 163840;

#define MAPV 0
DI void bh_of(int bid, int& b, int& h) { const int x = bid & 7, j = bid >> 3;
  if (MAPV == 0) { b = j >> 2; h = 4 * x + (j & 3); }
  else if (MAPV == 1) { h = 8 * (x & 3) + (j & 7); b = 4 * (x >> 2) + (j >> 3); }
  else if (MAPV == 2) { h = 16 * (x & 1) + (j & 15); b = 2 * (x >> 1) + (j >> 4); }
  else { h = j; b = x; } }

template <int N> DI void wait_vm() { asm volatile("s_waitcnt vmcnt(%0)" :: "i"(N) : "memory"); }
DI void wait_items(int n) {
  switch (n) { case 0: wait_vm<0>(); break; case 1: wait_vm<4>(); break; case 2: wait_vm<8>(); break; case 3: wait_vm<12>(); break;
               case 4: wait_vm<16>(); break; case 5: wait_vm<20>(); break; case 6: wait_vm<24>(); break; case 7: wait_vm<28>(); break; default: wait_vm<32>(); break; }
}
#define THR 8.0f
#define ABL_LDMAX 31
#define PIN(x) asm volatile("" : "+v"(x))

__global__ __launch_bounds__(512, 2) void attn_fused(const __hip_bfloat16* __restrict__ xin, const __hip_bfloat16* __restrict__ kc,
                                                     const __hip_bfloat16* __restrict__ vc, const __hip_bfloat16* __restrict__ wq,
                                                     u32x4* __restrict__ obuf) {
  extern __shared__ __attribute__((aligned(16))) char lds_raw[];
  const lds_ptr lds = (lds_ptr)lds_raw;
  const unsigned lds0 = rfl((unsigned)(uintptr_t)lds_raw);
  const int tid = threadIdx.x, lane = tid & 63, r32 = lane & 31, hi = lane >> 5;
  const int wid = rfl(tid >> 6), qi = wid & 3, grp = wid >> 2;
  STAMP_DECL; RSTAMP(60); STAMP(0);
  int b, h; bh_of(blockIdx.x, b, h);

  const int lr = (lane >> 2) & 7, lb = (lane >> 4) & 1;
  const char* kbase = (const char*)kc + (size_t)(b * NH + h) * SK * DH * 2;
  const char* vbase = (const char*)vc + (size_t)(b * NH + h) * SK * DH * 2 - SLOTB;
  const char* xbase = (const char*)xin + (size_t)b * SQ * DM * 2;
  const char* wbase = (const char*)wq + (size_t)h * DH * 2;
  unsigned voffK[4];
#pragma unroll
  for (int ii = 0; ii < 4; ++ii) { const int row = 16 * qi + 8 * (ii >> 1) + lr, xr = 2 * (ii >> 1) + lb, ch = 8 * (ii & 1) + 4 * hi + ((lane & 3) ^ xr);
    voffK[ii] = row * 256 + ch * 16; }
  const unsigned dq = lds0 + qi * 4096;
  auto issue_piece = [&](int u, unsigned pslot, int j) {
    glds16<KV_NT>(((j & 4) ? vbase : kbase) + (size_t)u * SLOTB, voffK[j & 3], dq + pslot + (j >> 2) * SLOTB + (j & 3) * 1024); };

  const unsigned xr_row = (r32 >> 2) & 3;
  const unsigned krd0 = 2048 * (r32 >> 3) + 64 * (r32 & 7) + 16 * ((0 + hi) ^ xr_row);
  const unsigned krd1 = 2048 * (r32 >> 3) + 64 * (r32 & 7) + 16 * ((2 + hi) ^ xr_row);
  const int tq = (lane & 15) >> 2, tp = lane & 3, blk = (lane >> 4) & 1;
  const int pch = 2 * blk + (tp & 1), phalf = tp >> 1;
  const unsigned vrd0 = SLOTB + 64 * (4 * hi + tq) + 16 * (pch ^ (0 + hi)) + 8 * phalf;
  const unsigned vrd1 = SLOTB + 2048 + 64 * (4 * hi + tq) + 16 * (pch ^ (2 + hi)) + 8 * phalf;

  bf16x8 qf[8];
  {
    unsigned voffQ[4];
#pragma unroll
    for (int ii = 0; ii < 4; ++ii) {
      if (!grp) { const int row = 32 * qi + 8 * ii + lr, xr = (2 * ii + lb) & 3, ch = 4 * hi + ((lane & 3) ^ xr); voffQ[ii] = row * (DM * 2) + ch * 16; }
      else { const int row = 16 * qi + 8 * (ii >> 1) + lr, xr = 2 * (ii >> 1) + lb, ch = 8 * (ii & 1) + 4 * hi + ((lane & 3) ^ xr); voffQ[ii] = row * (HD * 2) + ch * 16; }
    }
    const char* qsrc = grp ? wbase : xbase; const unsigned qstep = grp ? 64 * HD * 2 : 128; const unsigned dqq = dq + grp * SLOTB;
    auto issue_q = [&](int s, unsigned pslot, int ii) { glds16<false>(qsrc + (size_t)s * qstep, voffQ[ii], dqq + pslot + ii * 1024); };
    STAMP(32);
#pragma unroll
    for (int s = 0; s < 3; ++s) {
#pragma unroll
      for (int ii = 0; ii < 4; ++ii) issue_q(s, ((s + 2) % 3) * PAIRB, ii); }
    if (!grp) {
#pragma unroll
      for (int j = 0; j < 4; ++j) issue_piece(0, 3 * PAIRB, j);
    } else {
#pragma unroll
      for (int j = 0; j < 8; ++j) issue_piece(1, 4 * PAIRB, j);
    }
    STAMP(33);
    const unsigned xrd0 = 4096 * qi + 1024 * (r32 >> 3) + 64 * (r32 & 7) + 16 * ((0 + hi) ^ xr_row);
    const unsigned xrd1 = 4096 * qi + 1024 * (r32 >> 3) + 64 * (r32 & 7) + 16 * ((2 + hi) ^ xr_row);
    const unsigned wrd0 = SLOTB + grp * 1024 + 2048 * hi + 64 * (0 + tq) + 16 * (pch ^ (2 * hi + 0)) + 8 * phalf;
    const unsigned wrd1 = SLOTB + grp * 1024 + 2048 * hi + 64 * (4 + tq) + 16 * (pch ^ (2 * hi + 1)) + 8 * phalf;
    f32x16 qa0 = {}, qa1 = {};
    bf16x8 xA[4], wA[8], xB[4], wB[8];
#define QRD_X(X, p_, ks) X[ks] = lds_rd128((p_) + (((ks) & 1) ? xrd1 : xrd0) + 512 * ((ks) >> 1))
#define QRD_W(W, p_, ks, db) W[2 * (ks) + (db)] = cat8(lds_tr((p_) + wrd0 + 4096 * (ks) + 512 * (db)), lds_tr((p_) + wrd1 + 4096 * (ks) + 512 * (db)))
#define QITER(s, XC, WC, XN, WN) do { \
      if ((s) <= 1) { if (!grp) wait_vm<8>(); else wait_vm<12>(); } else if ((s) <= 7) wait_vm<4>(); else if ((s) == 8) { if (!grp) wait_vm<8>(); else wait_vm<0>(); } \
      BAR_LGKM(); STAMP(1 + (s)); SBAR(); \
      const unsigned pn_ = ps == 2 * PAIRB ? 0 : ps + PAIRB; const lds_cptr p_ = (lds_cptr)lds + pn_; \
      const bool rd_ = (s) < 9, ld_ = (s) + 3 <= 9, kv_ = (s) == 7 + grp; \
      _Pragma("unroll") for (int ks = 0; ks < 4; ++ks) { \
        qa0 = MFMA32(WC[2 * ks], XC[ks], qa0); SBAR(); \
        if (rd_) { QRD_X(XN, p_, ks); QRD_W(WN, p_, ks, 0); } \
        if (kv_) issue_piece((s) - 5, ps, 2 * ks); SBAR(); \
        qa1 = MFMA32(WC[2 * ks + 1], XC[ks], qa1); SBAR(); \
        if (rd_) QRD_W(WN, p_, ks, 1); \
        if (ld_) issue_q((s) + 3, ps, ks); \
        if (kv_) issue_piece((s) - 5, ps, 2 * ks + 1); SBAR(); } \
      PIN(qa0); PIN(qa1); ps = pn_; } while (0)
    unsigned ps = 2 * PAIRB;
    if (!grp) wait_vm<12>(); else wait_vm<16>();
    STAMP(34);
    BAR_LGKM(); STAMP(35);
    { const lds_cptr p_ = (lds_cptr)lds + ps;
#pragma unroll
      for (int ks = 0; ks < 4; ++ks) { QRD_X(xA, p_, ks); QRD_W(wA, p_, ks, 0); QRD_W(wA, p_, ks, 1); } }
    QITER(0, xA, wA, xB, wB); QITER(1, xB, wB, xA, wA); QITER(2, xA, wA, xB, wB); QITER(3, xB, wB, xA, wA); QITER(4, xA, wA, xB, wB);
    QITER(5, xB, wB, xA, wA); QITER(6, xA, wA, xB, wB); QITER(7, xB, wB, xA, wA); QITER(8, xA, wA, xB, wB); QITER(9, xB, wB, xA, wA);
#undef QITER
#pragma unroll
    for (int r = 0; r < 16; ++r) { qa0[r] *= SCL2E; qa1[r] *= SCL2E; }
    const bf16x8 o0 = pack8(qa0, 0), o1 = pack8(qa0, 1), o2 = pack8(qa1, 0), o3 = pack8(qa1, 1);
    const lds_ptr mine = lds + 2 * PAIRB + wid * 4096 + lane * 16; const lds_cptr theirs = (lds_cptr)lds + 2 * PAIRB + (wid ^ 4) * 4096 + lane * 16;
    *(LAS bf16x8*)(mine) = o0; *(LAS bf16x8*)(mine + 1024) = o1; *(LAS bf16x8*)(mine + 2048) = o2; *(LAS bf16x8*)(mine + 3072) = o3;
    BAR_LGKM();
    const bf16x8 t0 = lds_rd128(theirs), t1 = lds_rd128(theirs + 1024), t2 = lds_rd128(theirs + 2048), t3 = lds_rd128(theirs + 3072);
    if (!grp) { qf[0] = o0; qf[1] = o1; qf[2] = o2; qf[3] = o3; qf[4] = t0; qf[5] = t1; qf[6] = t2; qf[7] = t3; }
    else      { qf[0] = t0; qf[1] = t1; qf[2] = t2; qf[3] = t3; qf[4] = o0; qf[5] = o1; qf[6] = o2; qf[7] = o3; }
  }
  STAMP(11);

  f32x16 o[4] = {};
  f32x16 negm = {};
  float mref = 0.f, lrun = 0.f;
  bf16x8 pf[4], fr[16];
  f32x16 s0, s1;
  unsigned sl = 3 * PAIRB;
#define NXT(x) ((x) == 4 * PAIRB ? 0u : (x) + PAIRB)
#define PRV(x) ((x) == 0 ? 4 * PAIRB : (x) - PAIRB)
#define KRD(p_, i) lds_rd128((p_) + ((((i) >> 1) & 1) ? krd1 : krd0) + 512 * ((i) >> 2) + 8192 * ((i) & 1))
#define VRD(p_, kk, d) cat8(lds_tr((p_) + vrd0 + 4096 * (kk) + 512 * (d)), lds_tr((p_) + vrd1 + 4096 * (kk) + 512 * (d)))
#define READ_V4(p_, kk) do { _Pragma("unroll") for (int d = 0; d < 4; ++d) fr[4 * (kk) + d] = VRD(p_, kk, d); } while (0)
#define EXP2(x) __builtin_amdgcn_exp2f(x)
#define BLOCK_MM(p, DO_PV, DO_QK, ldmode) do { \
    const lds_cptr p_ = (lds_cptr)lds + sl; const unsigned dsl_ = PRV(sl); SBAR(); \
    _Pragma("unroll") for (int i = 0; i < 16; ++i) { \
      if (DO_PV) o[i & 3] = MFMA32(fr[i], pf[i >> 2], o[i & 3]); \
      SBAR(); \
      if (DO_QK) fr[i] = KRD(p_, i); \
      if ((i & 1) && ((ldmode) == 8 || ((ldmode) == 4 && i >= 8))) issue_piece((p) + 4, dsl_, i >> 1); \
      SBAR(); } \
    if (DO_QK) { s0 = MFMA32(fr[0], qf[0], negm); s1 = MFMA32(fr[1], qf[0], negm); \
      _Pragma("unroll") for (int s = 1; s < 8; ++s) { s0 = MFMA32(fr[2 * s], qf[s], s0); s1 = MFMA32(fr[2 * s + 1], qf[s], s1); } \
      PIN(s0); PIN(s1); } \
    if (DO_PV) { PIN(o[0]); PIN(o[1]); PIN(o[2]); PIN(o[3]); } \
    SBAR(); } while (0)
#define BLOCK_SM(first) do { \
    const lds_cptr vp_ = (lds_cptr)lds + sl; \
    READ_V4(vp_, 0); SBAR(); \
    float ma = MX3(s0[0], s0[1], s1[0]), mb = MX3(s0[2], s0[3], s1[1]); ma = MX3(ma, s1[2], s1[3]); \
    _Pragma("unroll") for (int r = 4; r < 16; r += 4) { ma = MX3(ma, s0[r], s0[r + 1]); mb = MX3(mb, s0[r + 2], s0[r + 3]); ma = MX3(ma, s1[r], s1[r + 1]); mb = MX3(mb, s1[r + 2], s1[r + 3]); } \
    const float ml = __builtin_fmaxf(ma, mb); \
    if ((first) || __builtin_expect(__any(ml > THR), 0)) { \
      const float rm = xhalf_max(ml); const float dl = (first) ? rm : __builtin_fmaxf(rm, 0.f); mref += dl; \
      _Pragma("unroll") for (int r = 0; r < 16; ++r) { s0[r] -= dl; s1[r] -= dl; negm[r] = -mref; } \
      if (!(first)) { const float f = __builtin_amdgcn_exp2f(-dl); lrun *= f; \
        _Pragma("unroll") for (int d = 0; d < 4; ++d) _Pragma("unroll") for (int r = 0; r < 16; ++r) o[d][r] *= f; } } \
    float sa = 0.f, sb = 0.f; \
    READ_V4(vp_, 1); SBAR(); \
    _Pragma("unroll") for (int r = 0; r < 8; ++r) { s0[r] = EXP2(s0[r]); sa += s0[r]; } \
    pf[0] = pack8(s0, 0); PIN(pf[0]); PIN(sa); SBAR(); \
    READ_V4(vp_, 2); SBAR(); \
    _Pragma("unroll") for (int r = 8; r < 16; ++r) { s0[r] = EXP2(s0[r]); sa += s0[r]; } \
    pf[1] = pack8(s0, 1); PIN(pf[1]); PIN(sa); SBAR(); \
    READ_V4(vp_, 3); SBAR(); \
    _Pragma("unroll") for (int r = 0; r < 16; ++r) { s1[r] = EXP2(s1[r]); sb += s1[r]; } \
    pf[2] = pack8(s1, 0); pf[3] = pack8(s1, 1); lrun += sa + sb; PIN(pf[2]); PIN(pf[3]); PIN(lrun); SBAR(); } while (0)

  if (!grp) {
    wait_vm<8>(); BAR_LGKM(); STAMP(12);
    BLOCK_MM(0, false, true, 8); sl = NXT(sl);
    BAR_LGKM(); STAMP(13);
    BLOCK_SM(true); sl = NXT(sl);
#pragma unroll 1
    for (int p = 2; p <= 10; p += 2) {
      wait_vm<8>(); BAR_LGKM(); STAMP(12 + p);
      BLOCK_MM(p, true, true, 8); sl = NXT(sl);
      BAR_LGKM(); STAMP(13 + p);
      BLOCK_SM(false); sl = NXT(sl);
    }
    wait_vm<8>(); BAR_LGKM(); STAMP(24);
    BLOCK_MM(12, true, true, 4); sl = NXT(sl);
    BAR_LGKM(); STAMP(25);
    BLOCK_SM(false); sl = NXT(sl);
    wait_vm<4>(); BAR_LGKM(); STAMP(26);
    BLOCK_MM(14, true, true, 0); sl = NXT(sl);
    BAR_LGKM(); STAMP(27);
    BLOCK_SM(false); sl = NXT(sl);
    wait_vm<0>(); BAR_LGKM(); STAMP(28);
    BLOCK_MM(16, true, false, 0);
    BAR_LGKM(); STAMP(29);
  } else {
    BAR_LGKM(); STAMP(12); sl = NXT(sl);
    wait_vm<8>(); BAR_LGKM(); STAMP(13);
    BLOCK_MM(1, false, true, 8); sl = NXT(sl);
    BAR_LGKM(); STAMP(14);
    BLOCK_SM(true); sl = NXT(sl);
#pragma unroll 1
    for (int p = 3; p <= 11; p += 2) {
      wait_vm<8>(); BAR_LGKM(); STAMP(12 + p);
      BLOCK_MM(p, true, true, 8); sl = NXT(sl);
      BAR_LGKM(); STAMP(13 + p);
      BLOCK_SM(false); sl = NXT(sl);
    }
    wait_vm<8>(); BAR_LGKM(); STAMP(25);
    BLOCK_MM(13, true, true, 0); sl = NXT(sl);
    BAR_LGKM(); STAMP(26);
    BLOCK_SM(false); sl = NXT(sl);
    wait_vm<0>(); BAR_LGKM(); STAMP(27);
    BLOCK_MM(15, true, true, 0); sl = NXT(sl);
    BAR_LGKM(); STAMP(28);
    BLOCK_SM(false); sl = NXT(sl);
    BAR_LGKM(); STAMP(29);
    BLOCK_MM(17, true, false, 0);
  }
  STAMP(30);
  lrun = xhalf_sum(lrun);
  {
    const lds_ptr mine = lds + wid * 8192 + lane * 16; const lds_cptr theirs = (lds_cptr)lds + (wid ^ 4) * 8192 + lane * 16;
    const f32x16& g0 = grp ? o[0] : o[2]; const f32x16& g1 = grp ? o[1] : o[3];
#pragma unroll
    for (int i = 0; i < 4; ++i) {
      *(LAS f32x4*)(mine + i * 1024) = (f32x4){g0[4 * i], g0[4 * i + 1], g0[4 * i + 2], g0[4 * i + 3]};
      *(LAS f32x4*)(mine + 4096 + i * 1024) = (f32x4){g1[4 * i], g1[4 * i + 1], g1[4 * i + 2], g1[4 * i + 3]};
    }
    *(LAS f32x2*)(lds + 65536 + wid * 512 + lane * 8) = (f32x2){mref, lrun};
    BAR_LGKM();
    const f32x2 st = *(const LAS f32x2*)(lds + 65536 + (wid ^ 4) * 512 + lane * 8);
    const float mo = st[0], lo = st[1];
    const float m = __builtin_fmaxf(mref, mo);
    const float fa = __builtin_amdgcn_exp2f(mref - m), fb = __builtin_amdgcn_exp2f(mo - m);
    const float inv = 1.f / (fa * lrun + fb * lo);
    const float ca = fa * inv, cb = fb * inv;
    f32x16 k0 = grp ? o[2] : o[0], k1 = grp ? o[3] : o[1];
#pragma unroll
    for (int i = 0; i < 4; ++i) {
      const f32x4 t0 = *(const LAS f32x4*)(theirs + i * 1024), t1 = *(const LAS f32x4*)(theirs + 4096 + i * 1024);
#pragma unroll
      for (int e = 0; e < 4; ++e) { k0[4 * i + e] = k0[4 * i + e] * ca + t0[e] * cb; k1[4 * i + e] = k1[4 * i + e] * ca + t1[e] * cb; }
    }
    u32x4* dst = obuf + ((size_t)((b * NH + h) * 4 + qi) * 8 + 4 * grp) * 64 + lane;
    dst[0] = __builtin_bit_cast(u32x4, pack8(k0, 0)); dst[64] = __builtin_bit_cast(u32x4, pack8(k0, 1));
    dst[128] = __builtin_bit_cast(u32x4, pack8(k1, 0)); dst[192] = __builtin_bit_cast(u32x4, pack8(k1, 1));
  }
  STAMP(31); RSTAMP(61); STAMP_FLUSH();
}

__global__ __launch_bounds__(512, 2) void oproj_partial(const u32x4* __restrict__ obuf, const __hip_bfloat16* __restrict__ wo, float* __restrict__ part) {
  extern __shared__ __attribute__((aligned(16))) char lds_raw[];
  const lds_ptr lds = (lds_ptr)lds_raw;
  const unsigned lds0 = rfl((unsigned)(uintptr_t)lds_raw);
  const int tid = threadIdx.x, lane = tid & 63, r32 = lane & 31, hi = lane >> 5;
  const int wid = rfl(tid >> 6), qi = wid & 3, kh = wid >> 2;
  int b, h; bh_of(blockIdx.x, b, h);
  const int g = h >> 2, r = h & 3;
  const char* wbase = (const char*)wo + ((size_t)512 * g * DM + 160 * r) * 2;
#pragma unroll 4
  for (int i = 0; i < 20; ++i) {
    const int piece = wid * 20 + i, c = piece * 64 + lane, row = c / 20, ch = c - row * 20;
    glds16<false>(wbase, row * (DM * 2) + ch * 16, lds0 + piece * 1024);
  }
  bf16x8 of[16];
#pragma unroll
  for (int i = 0; i < 16; ++i) { const int hh = 4 * g + 2 * kh + (i >> 3);
    of[i] = __builtin_bit_cast(bf16x8, obuf[((size_t)((b * NH + hh) * 4 + qi) * 8 + (i & 7)) * 64 + lane]); }
  WAIT_BAR(0);
  const int tq = (lane & 15) >> 2, tp = lane & 3, blk = (lane >> 4) & 1;
  const lds_cptr wr = (lds_cptr)lds + (256 * kh + 8 * hi + tq) * 320 + 32 * blk + 8 * tp;
  f32x16 acc[5] = {};
#pragma unroll
  for (int i = 0; i < 16; ++i) {
#pragma unroll
    for (int nt = 0; nt < 5; ++nt) {
      const bf16x8 a = cat8(lds_tr(wr + i * 16 * 320 + nt * 64), lds_tr(wr + (i * 16 + 4) * 320 + nt * 64));
      acc[nt] = MFMA32(a, of[i], acc[nt]);
    }
  }
  float* dst = part + ((size_t)(b * 16 + 2 * g + kh) * SQ + 32 * qi + r32) * DM + 160 * r + 4 * hi;
#pragma unroll
  for (int nt = 0; nt < 5; ++nt)
#pragma unroll
    for (int i = 0; i < 4; ++i)
      *(f32x4*)(dst + 32 * nt + 8 * i) = (f32x4){acc[nt][4 * i], acc[nt][4 * i + 1], acc[nt][4 * i + 2], acc[nt][4 * i + 3]};
}

__global__ __launch_bounds__(256) void oproj_reduce(const float* __restrict__ part, u32x2* __restrict__ out) {
  const int i = blockIdx.x * 256 + threadIdx.x;
  const int b = i / (SQ * DM / 4), rem = i - b * (SQ * DM / 4);
  const f32x4* p = (const f32x4*)part + (size_t)b * 16 * (SQ * DM / 4) + rem;
  f32x4 s = p[0];
#pragma unroll
  for (int k = 1; k < 16; ++k) s += p[(size_t)k * (SQ * DM / 4)];
  out[i] = (u32x2){cvtpk(s[0], s[1]), cvtpk(s[2], s[3])};
}

extern "C" void kernel_launch(void* const* d_in, const int* in_sizes, int n_in, void* d_out, int out_size, void* d_ws, size_t ws_size, hipStream_t stream) {
  const __hip_bfloat16* x = (const __hip_bfloat16*)d_in[0];
  const __hip_bfloat16* kc = (const __hip_bfloat16*)d_in[1];
  const __hip_bfloat16* vc = (const __hip_bfloat16*)d_in[2];
  const __hip_bfloat16* wq = (const __hip_bfloat16*)d_in[3];
  const __hip_bfloat16* wo = (const __hip_bfloat16*)d_in[4];
  u32x4* obuf = (u32x4*)d_ws;
  float* part = (float*)((char*)d_ws + (size_t)NB * NH * SQ * DH * 2);
  (void)hipFuncSetAttribute((const void*)attn_fused, hipFuncAttributeMaxDynamicSharedMemorySize, LDS_BYTES);
  (void)hipFuncSetAttribute((const void*)oproj_partial, hipFuncAttributeMaxDynamicSharedMemorySize, LDS_BYTES);
  hipLaunchKernelGGL(attn_fused, dim3(256), dim3(512), LDS_BYTES, stream, x, kc, vc, wq, obuf);
  hipLaunchKernelGGL(oproj_partial, dim3(256), dim3(512), LDS_BYTES, stream, (const u32x4*)obuf, wo, part);
  hipLaunchKernelGGL(oproj_reduce, dim3(NB * SQ * DM / 4 / 256), dim3(256), 0, stream, (const float*)part, (u32x2*)d_out);
}
```

```cpp
#include <hip/hip_runtime.h>
#include <hip/hip_bf16.h>
#include <cstdint>

typedef float    f32x4  __attribute__((ext_vector_type(4)));
typedef float    f32x2  __attribute__((ext_vector_type(2)));
typedef __bf16   bf16x8 __attribute__((ext_vector_type(8)));
typedef __bf16   bf16x2 __attribute__((ext_vector_type(2)));
typedef short    s16x4  __attribute__((ext_vector_type(4)));
typedef short    s16x8  __attribute__((ext_vector_type(8)));
typedef unsigned u32x4  __attribute__((ext_vector_type(4)));
typedef unsigned u32x2  __attribute__((ext_vector_type(2)));
#define DI __device__ __forceinline__
#define LAS __attribute__((address_space(3)))
typedef LAS const char* lds_cptr;
typedef LAS char* lds_ptr;

constexpr int SQ = 128, DM = 640, NH = 32, SK = 1024, DH = 128;
constexpr int HD = NH * DH;
constexpr float SCL2E = 0.08838834764831845f * 1.4426950408889634f;
constexpr unsigned PAIRB = 32768, SLOTB = 16384;
constexpr int LDS_BYTES = 163840;
constexpr unsigned WS_MBOX = 0, MBOXB = 1024;
constexpr unsigned MB_F2 = 512, MB_REQ = 640;
constexpr unsigned WS_OBUF = 1u << 20;
constexpr unsigned WS_PBUF = 16u << 20;
constexpr unsigned WS_BYTES = 32u << 20;
constexpr int AUX_SC1 = 16;
constexpr unsigned SPIN_CAP = 1u << 16;

#define MFMA16(a, b, c) __builtin_amdgcn_mfma_f32_16x16x32_bf16((a), (b), (c), 0, 0, 0)
#define SBAR() __builtin_amdgcn_sched_barrier(0)
#define BAR_LGKM() asm volatile("s_waitcnt lgkmcnt(0)\n\ts_barrier" ::: "memory")
#define PIN(x) asm volatile("" : "+v"(x))
#define EXP2(x) __builtin_amdgcn_exp2f(x)
#define MX3(a, b, c) __builtin_fmaxf(__builtin_fmaxf((a), (b)), (c))
#define THR 8.0f
#define PACE 113
#define ODIST 6
#define NT1 3

DI int lane_id_opaque() { int l; asm volatile("v_mbcnt_lo_u32_b32 %0, -1, 0\n\tv_mbcnt_hi_u32_b32 %0, -1, %0" : "=v"(l)); return l; }
DI unsigned rfl(unsigned x) { return __builtin_amdgcn_readfirstlane(x); }
DI unsigned cvtpk(float lo, float hi) { f32x2 v = {lo, hi}; bf16x2 b = __builtin_convertvector(v, bf16x2); return __builtin_bit_cast(unsigned, b); }
template <int N> DI void wait_vm() { asm volatile("s_waitcnt vmcnt(%0)" :: "i"(N) : "memory"); }

template <bool NT>
DI void glds16(const void* sbase, unsigned voff, unsigned lds_base) {
  if constexpr (NT)
    asm volatile("s_mov_b32 m0, %2\n\ts_nop 0\n\tglobal_load_lds_dwordx4 %0, %1 nt" :: "v"(voff), "s"(sbase), "s"(lds_base) : "memory");
  else
    asm volatile("s_mov_b32 m0, %2\n\ts_nop 0\n\tglobal_load_lds_dwordx4 %0, %1" :: "v"(voff), "s"(sbase), "s"(lds_base) : "memory");
}
DI unsigned gload4_sc1(const void* sbase, unsigned voff) { unsigned r; asm volatile("global_load_dword %0, %1, %2 sc1" : "=v"(r) : "v"(voff), "s"(sbase) : "memory"); return r; }

DI bf16x8 lds_rd128(lds_cptr p) { return *(const LAS bf16x8*)p; }
typedef short v4i16_t __attribute__((ext_vector_type(4)));
DI s16x4 lds_tr(lds_cptr p) { return __builtin_bit_cast(s16x4, __builtin_amdgcn_ds_read_tr16_b64_v4i16((LAS v4i16_t*)p)); }
DI bf16x8 cat8(s16x4 lo, s16x4 hi) { s16x8 v = __builtin_shufflevector(lo, hi, 0, 1, 2, 3, 4, 5, 6, 7); return __builtin_bit_cast(bf16x8, v); }
DI float xhalf_max(float m) { auto rr = __builtin_amdgcn_permlane32_swap(__float_as_uint(m), __float_as_uint(m), false, false); return __builtin_fmaxf(__uint_as_float(rr[0]), __uint_as_float(rr[1])); }
DI float row_max(float m) { m = xhalf_max(m); return __builtin_fmaxf(m, __shfl_xor(m, 16)); }
DI float xhalf_sum(float m) { auto rr = __builtin_amdgcn_permlane32_swap(__float_as_uint(m), __float_as_uint(m), false, false); return __uint_as_float(rr[0]) + __uint_as_float(rr[1]); }

DI float row_sum(float m) { m = xhalf_sum(m); return m + __shfl_xor(m, 16); }

struct Args { const char* x; const char* kc; const char* vc; const char* wq; const char* wo; char* out; char* ws; };

template <int G>
DI void body(const Args& a, lds_ptr lds, const unsigned lds0, const int wid) {
  const int lane = lane_id_opaque(), hi = lane >> 5, qi = wid & 3;
  const int xg = blockIdx.x & 7, jj = blockIdx.x >> 3;
  const int g = xg & 3, r = jj & 7, h = 8 * g + r, b = 4 * (xg >> 2) + (jj >> 3), bh = b * NH + h;

  const int lr = (lane >> 2) & 7, lb = (lane >> 4) & 1;
  const char* kbase = a.kc + (size_t)bh * SK * DH * 2;
  const char* vbase = a.vc + (size_t)bh * SK * DH * 2;
  const int trot = (jj + 2 * xg) & 15;
  const unsigned voffK = (16 * qi + lr) * 256 + (4 * hi + ((lane & 3) ^ (2 * lb))) * 16;
  const unsigned dq = lds0 + qi * 4096;
  auto piece_src = [&](int u, int j) -> const char* { return ((j & 4) ? vbase : kbase) + (size_t)((u - (j >> 2) + trot) & 15) * SLOTB; };
  auto issue_piece = [&](int u, unsigned pslot, int j) { glds16<true>(piece_src(u, j) + ((j >> 1) & 1) * 2048 + (j & 1) * 128, voffK, dq + pslot + (j >> 2) * SLOTB + (j & 3) * 1024); };

  const int tq = (lane & 15) >> 2, tp = lane & 3;
  const unsigned S1 = 2 * PAIRB + SLOTB;

  const __amdgpu_buffer_rsrc_t rs = __builtin_amdgcn_make_buffer_rsrc((void*)a.ws, 0, (int)WS_BYTES, 0x00020000);
  bf16x8 qf[8];
  unsigned nonce;
  {
    const char* qsrc = G ? a.wq + (size_t)h * DH * 2 : a.x + (size_t)b * SQ * DM * 2;
    constexpr unsigned qstep = G ? 64 * HD * 2 : 128;
    unsigned voffQ[4];
#pragma unroll
    for (int ii = 0; ii < 4; ++ii) {
      if constexpr (G == 0) { const int row = 32 * qi + 8 * ii + lr, ch = 4 * hi + ((lane & 3) ^ (2 * lb)); voffQ[ii] = row * (DM * 2) + ch * 16; }
      else { const int row = 16 * qi + 8 * (ii >> 1) + lr, ch = 8 * (ii & 1) + 4 * hi + ((lane & 3) ^ (2 * (ii >> 1))); voffQ[ii] = row * (HD * 2) + ch * 16; }
    }
    unsigned mb0 = 0, mb1 = 0, mb2 = 0;
#define PS(s) (((s) % 3 == 0 ? 1 : (s) % 3 == 1 ? 4 : 0) * PAIRB)
    const int rot = (r + 3 * (jj >> 3)) % 10;
    auto issue_q = [&](int s, int i) { const unsigned sr = rfl(s + rot >= 10 ? s + rot - 10 : s + rot); glds16<false>(qsrc + (size_t)sr * qstep, voffQ[i], dq + PS(s) + G * SLOTB + i * 1024); };

#pragma unroll
    for (int i = 0; i < 4; ++i) issue_q(0, i);
    if constexpr (G == 0) { const char* mb = a.ws + WS_MBOX + bh * MBOXB; mb0 = gload4_sc1(mb, lane * 4); mb1 = gload4_sc1(mb, 256 + lane * 4); mb2 = gload4_sc1(mb, 512 + lane * 4); }
#pragma unroll
    for (int i = 0; i < 4; ++i) issue_q(1, i);

    const int qh = wid & 1, db = wid >> 1;
    const int g4q = lane >> 4;
    const unsigned xrd = 8192 * qh + 1024 * ((lane & 15) >> 3) + 64 * (lane & 7) + 16 * (g4q ^ (2 * ((lane >> 2) & 1)));
    const unsigned wrdE = SLOTB + db * 512 + 2048 * g4q + 64 * tq + 16 * ((0 + (tp >> 1)) ^ (2 * (g4q & 1))) + 8 * (tp & 1);
    const unsigned wrdO = SLOTB + db * 512 + 2048 * g4q + 64 * tq + 16 * ((2 + (tp >> 1)) ^ (2 * (g4q & 1))) + 8 * (tp & 1);
    f32x4 qa[2][4] = {};
    bf16x8 xA[8], wA[4], xB[8], wB[4];
#define QRD_X(X, p_, i) X[i] = lds_rd128((p_) + xrd + 512 * ((i) >> 2) + 2048 * ((i) & 3))
#define QRD_W(W, p_, i) W[i] = cat8(lds_tr((p_) + (((i) & 1) ? wrdO : wrdE) + 8192 * ((i) >> 1)), lds_tr((p_) + (((i) & 1) ? wrdO : wrdE) + 8192 * ((i) >> 1) + 256))
#define QGAP(s, j) do { \
      if constexpr ((s) + 3 <= 9) issue_q((s) + 3, j); \
      else if constexpr ((s) == 7) { if constexpr (G == 0) issue_piece(0, 2 * PAIRB, j); issue_piece(1, 3 * PAIRB, 4 * G + (j)); } \
      } while (0)
#define QK2(s, j, XC, WC, XN, WN) do {     \
        qa[(j) & 1][0] = MFMA16(WC[j], XC[4 * ((j) >> 1)], qa[(j) & 1][0]); qa[(j) & 1][1] = MFMA16(WC[j], XC[4 * ((j) >> 1) + 1], qa[(j) & 1][1]); SBAR(); \
        if constexpr ((s) < 9) { QRD_W(WN, p_, j); QRD_X(XN, p_, 2 * (j)); } \
        SBAR(); \
        qa[(j) & 1][2] = MFMA16(WC[j], XC[4 * ((j) >> 1) + 2], qa[(j) & 1][2]); qa[(j) & 1][3] = MFMA16(WC[j], XC[4 * ((j) >> 1) + 3], qa[(j) & 1][3]); SBAR(); \
        if constexpr ((s) < 9) QRD_X(XN, p_, 2 * (j) + 1); \
        QGAP(s, j); SBAR(); } while (0)
#define QITER(s, XC, WC, XN, WN) do { \
      if constexpr ((s) <= 7) wait_vm<4>(); else if constexpr ((s) == 8) wait_vm<G ? 4 : 8>(); \
      BAR_LGKM(); SBAR(); \
      const lds_cptr p_ = (lds_cptr)lds + PS((s) + 1); \
      QK2(s, 0, XC, WC, XN, WN); QK2(s, 1, XC, WC, XN, WN); QK2(s, 2, XC, WC, XN, WN); QK2(s, 3, XC, WC, XN, WN); \
      _Pragma("unroll") for (int i_ = 0; i_ < 8; ++i_) PIN(qa[i_ >> 2][i_ & 3]); } while (0)
    wait_vm<G ? 4 : 7>();
    BAR_LGKM();
    { const lds_cptr p_ = (lds_cptr)lds + PS(0);
#pragma unroll
      for (int j = 0; j < 4; ++j) { QRD_W(wA, p_, j); QRD_X(xA, p_, 2 * j); QRD_X(xA, p_, 2 * j + 1); } }
    SBAR();
#pragma unroll
    for (int i = 0; i < 4; ++i) issue_q(2, i);
    QITER(0, xA, wA, xB, wB);
    if constexpr (G == 0) {
      PIN(mb0); PIN(mb1); PIN(mb2);
      if (wid == 0) {
        unsigned n = (unsigned)__builtin_amdgcn_readlane((int)mb2, 32) + 1u;
        while (__any(mb0 == n || mb1 == n || mb2 == n)) ++n;
        if (lane == 0) { __hip_atomic_store((unsigned*)(a.ws + WS_MBOX + bh * MBOXB + MB_REQ), n, __ATOMIC_RELAXED, __HIP_MEMORY_SCOPE_AGENT); *(LAS unsigned*)(lds + S1) = n; }
      }
    }
    QITER(1, xB, wB, xA, wA); QITER(2, xA, wA, xB, wB);
    nonce = rfl(*(const LAS unsigned*)(lds + S1));
    QITER(3, xB, wB, xA, wA); QITER(4, xA, wA, xB, wB);
    QITER(5, xB, wB, xA, wA); QITER(6, xA, wA, xB, wB);
    QITER(7, xB, wB, xA, wA); QITER(8, xA, wA, xB, wB); QITER(9, xB, wB, xA, wA);
#undef QITER
#undef QGAP
    const lds_ptr ex = lds + ((4 * qh * 4 + db) * 4 + (g4q >> 1)) * 256 + (lane & 15) * 16 + 8 * (g4q & 1);
#pragma unroll
    for (int i = 0; i < 8; ++i) { const f32x4 v = qa[i >> 2][i & 3] * SCL2E;
      *(LAS u32x2*)(ex + (i & 3) * 4096 + (i >> 2) * 512) = (u32x2){cvtpk(v[0], v[1]), cvtpk(v[2], v[3])}; }
  }
  const int g4l = lane >> 4;
  const unsigned krd = 2048 * ((lane & 15) >> 3) + 64 * (lane & 7) + 16 * (g4l ^ (2 * ((lane >> 2) & 1)));
  const unsigned vrdE = SLOTB + 2048 * (g4l >> 1) + 64 * (4 * (g4l & 1) + tq) + 16 * ((0 + (tp >> 1)) ^ (2 * (g4l & 1))) + 8 * (tp & 1);
  const unsigned vrdO = SLOTB + 2048 * (g4l >> 1) + 64 * (4 * (g4l & 1) + tq) + 16 * ((2 + (tp >> 1)) ^ (2 * (g4l & 1))) + 8 * (tp & 1);
  const char* wobase = a.wo + ((size_t)1024 * g * DM + 80 * r) * 2;
  auto issue_wo = [&](int P) {
    const unsigned c = 64 * P + lane, row = (c * 6554u) >> 16, ch = c - row * 10;
    glds16<false>(wobase, row * (DM * 2) + ch * 16, lds0 + P * 1024); };
  unsigned wdummy = 0;
  auto warm_wo = [&](int p) {
    unsigned ln = (p - 1) * 896 + (jj * 4 + qi) * 7 + (lane < 7 ? lane : 0); ln = ln < 10240 ? ln : 0;
    asm volatile("global_load_dword %0, %1, %2" : "+v"(wdummy) : "v"(ln * 128), "s"(a.wo + (size_t)1024 * g * DM * 2) : "memory"); };
  f32x4 o[8][2] = {};
  f32x4 negm[2] = {};
  float mref[2] = {0.f, 0.f}, lrun[2] = {0.f, 0.f};
  bf16x8 pf[2][2], fr[16];
  f32x4 sc[4][2];
  u32x4 wl[13];
#define SLOT(u) ((unsigned)((0xd46a351a8d111aull >> (3 * (u))) & 7) * PAIRB)
  auto pair_at = [&](int p) -> int { return p + 2; };
#define KRD(p_, i) lds_rd128((p_) + krd + 512 * ((i) >> 2) + 4096 * ((i) & 3))
#define VRD(p_, i) cat8(lds_tr((p_) + (((i) & 1) ? vrdO : vrdE) + 512 * (((i) >> 1) & 3) + 8192 * ((i) >> 3)), lds_tr((p_) + (((i) & 1) ? vrdO : vrdE) + 512 * (((i) >> 1) & 3) + 8192 * ((i) >> 3) + 4096))
#define READ_V4(p_, j) do { _Pragma("unroll") for (int d = 0; d < 4; ++d) fr[4 * (j) + d] = VRD(p_, 4 * (j) + d); } while (0)
#define PK2(A, B) __builtin_bit_cast(bf16x8, (u32x4){cvtpk(A[0], A[1]), cvtpk(A[2], A[3]), cvtpk(B[0], B[1]), cvtpk(B[2], B[3])})
#define EXPE(kt, qt, r) do { sc[kt][qt][r] = EXP2(sc[kt][qt][r]); lrun[qt] += sc[kt][qt][r]; } while (0)
#define LDP(LD, P, DSL, HF, j, WB, NW) do { if ((LD) == 1) issue_piece(u_, DSL, 4 * (HF) + (j)); \
    if ((LD) == 2 && (j) < (NW)) issue_wo((WB) + (NW) * qi + (j)); \
    if ((LD) == 3) { _Pragma("unroll") for (int i_ = (13 * (j)) / 4; i_ < (13 * ((j) + 1)) / 4; ++i_) { const int P_ = i_ < 8 ? 96 + 8 * qi + i_ : 140 + 5 * qi + (i_ - 8); \
      const unsigned c_ = 64 * P_ + lane, row_ = (c_ * 6554u) >> 16, ch_ = c_ - row_ * 10; const u32x4* q_ = (const u32x4*)(wobase + row_ * (DM * 2) + ch_ * 16); \
      wl[i_] = *q_; } } } while (0)
#define WO1(LD, P) do { if ((LD) == 1 && (P) >= 5 && (P) <= 12) issue_wo(8 * ((P) - 5) + wid); } while (0)
#define BLOCK_MM(SL, DO_PV, DO_QK, LD, P, WB, NW) do { \
    const lds_cptr p_ = (lds_cptr)lds + (SL); const int u_ = pair_at(P); const unsigned DSL = SLOT(u_); \
    __builtin_amdgcn_s_setprio(1); SBAR(); \
    _Pragma("unroll") for (int i = 0; i < 16; ++i) { \
      if (DO_PV) { o[i & 7][0] = MFMA16(fr[i], pf[i >> 3][0], o[i & 7][0]); o[i & 7][1] = MFMA16(fr[i], pf[i >> 3][1], o[i & 7][1]); } \
      SBAR(); \
      if (DO_QK) fr[i] = KRD(p_, i); \
      if ((DO_PV) && (DO_QK)) {     \
        if (i < 6) { _Pragma("unroll") for (int e = 3 * i; e < 3 * i + 3 && e < 16; ++e) EXPE(2 + ((e >> 2) & 1), e >> 3, e & 3); \
          PIN(sc[2][0]); PIN(sc[3][0]); PIN(sc[2][1]); PIN(sc[3][1]); PIN(lrun[0]); PIN(lrun[1]); } \
        if (i == 6) { pf[1][0] = PK2(sc[2][0], sc[3][0]); PIN(pf[1][0]); } \
        if (i == 7) { pf[1][1] = PK2(sc[2][1], sc[3][1]); PIN(pf[1][1]); } } \
      SBAR(); } \
    if (DO_QK) { \
      _Pragma("unroll") for (int i = 0; i < 16; ++i) { \
        sc[i & 3][0] = MFMA16(fr[i], qf[i >> 2], (i >> 2) ? sc[i & 3][0] : negm[0]); sc[i & 3][1] = MFMA16(fr[i], qf[4 + (i >> 2)], (i >> 2) ? sc[i & 3][1] : negm[1]); SBAR(); \
        if (i == 0) { WO1(LD, P); SBAR(); } \
        if ((i & 3) == 2) { LDP(LD, P, DSL, 0, i >> 2, WB, NW); SBAR(); } } \
      _Pragma("unroll") for (int k = 0; k < 4; ++k) { PIN(sc[k][0]); PIN(sc[k][1]); } } \
    if (DO_PV) { _Pragma("unroll") for (int d = 0; d < 8; ++d) { PIN(o[d][0]); PIN(o[d][1]); } } \
    __builtin_amdgcn_s_setprio(0); SBAR(); } while (0)
#define MAX16(qt) MX3(MX3(MX3(sc[0][qt][0], sc[0][qt][1], sc[0][qt][2]), MX3(sc[0][qt][3], sc[1][qt][0], sc[1][qt][1]), MX3(sc[1][qt][2], sc[1][qt][3], sc[2][qt][0])), \
                      MX3(sc[2][qt][1], sc[2][qt][2], sc[2][qt][3]), MX3(MX3(sc[3][qt][0], sc[3][qt][1], sc[3][qt][2]), sc[3][qt][3], sc[3][qt][3]))
#define BLOCK_SM(SL, first, LD, P, WB, NW) do { \
    const lds_cptr vp_ = (lds_cptr)lds + (SL); const int u_ = pair_at(P); const unsigned DSL = SLOT(u_); \
    WO1(LD, P); if ((LD) == 1 && (P) <= 12) warm_wo(P); LDP(LD, P, DSL, 1, 0, WB, NW); \
    READ_V4(vp_, 0); SBAR(); \
    const float ml0 = MAX16(0), ml1 = MAX16(1); \
    if ((first) || __builtin_expect(__any(__builtin_fmaxf(ml0, ml1) > THR), 0)) {     \
      _Pragma("unroll") for (int qt = 0; qt < 2; ++qt) { \
        const float rm = row_max(qt ? ml1 : ml0); const float dl = (first) ? rm : __builtin_fmaxf(rm, 0.f); mref[qt] += dl; \
        _Pragma("unroll") for (int k = 0; k < 4; ++k) { sc[k][qt] -= dl; negm[qt][k] = -mref[qt]; } \
        if (!(first)) { const float f = EXP2(-dl); lrun[qt] *= f; _Pragma("unroll") for (int d = 0; d < 8; ++d) o[d][qt] *= f; } } } \
    LDP(LD, P, DSL, 1, 1, WB, NW); \
    READ_V4(vp_, 1); SBAR(); \
    _Pragma("unroll") for (int e = 0; e < 8; ++e) EXPE(e >> 2, 0, e & 3); \
    pf[0][0] = PK2(sc[0][0], sc[1][0]); PIN(pf[0][0]); PIN(lrun[0]); SBAR(); \
    LDP(LD, P, DSL, 1, 2, WB, NW); \
    READ_V4(vp_, 2); SBAR(); \
    _Pragma("unroll") for (int e = 0; e < 8; ++e) EXPE(e >> 2, 1, e & 3); \
    pf[0][1] = PK2(sc[0][1], sc[1][1]); PIN(pf[0][1]); PIN(lrun[1]); SBAR(); \
    LDP(LD, P, DSL, 1, 3, WB, NW); \
    READ_V4(vp_, 3); SBAR(); \
    if ((LD) != 1) {     \
      _Pragma("unroll") for (int e = 0; e < 16; ++e) EXPE(2 + ((e >> 2) & 1), e >> 3, e & 3); \
      pf[1][0] = PK2(sc[2][0], sc[3][0]); pf[1][1] = PK2(sc[2][1], sc[3][1]); PIN(pf[1][0]); PIN(pf[1][1]); PIN(lrun[0]); PIN(lrun[1]); } \
    PIN(sc[2][0]); PIN(sc[3][0]); PIN(sc[2][1]); PIN(sc[3][1]); SBAR(); } while (0)

#define PEER(l) (b * NH + (((l) < 12 ? (l) : 0) < 8 ? 8 * g + ((l) < 12 ? (l) : 0) : 8 * ((l) - 8) + r))
#define LATE_CONSTS const int ln = lane_id_opaque(); \
  const unsigned SC = 3 * PAIRB + qi * 8192 + ln * 16;       \
  const unsigned ST = 140 * 1024 + qi * 1024 + ln * 16;      \
  req = __builtin_amdgcn_raw_buffer_load_b32(rs, WS_MBOX + PEER(ln) * MBOXB + MB_REQ, 0, AUX_SC1)
#define RELAY(NBLK, KB, L) do {     \
    const unsigned wa_ = ((L) >> 2) * 160 + 8 * ((L) & 3), wb_ = wa_ + 16 * 160; \
    bf16x8 t_[NBLK][5]; \
    _Pragma("unroll") for (int k = 0; k < (NBLK); ++k) _Pragma("unroll") for (int nt = 0; nt < 5; ++nt) \
      t_[k][nt] = cat8(lds_tr((lds_cptr)lds + (KB) * 5120 + nt * 32 + wa_), lds_tr((lds_cptr)lds + (KB) * 5120 + nt * 32 + wb_)); \
    asm volatile("s_waitcnt lgkmcnt(0)" ::: "memory"); \
    _Pragma("unroll") for (int k = 0; k < (NBLK); ++k) _Pragma("unroll") for (int nt = 0; nt < 5; ++nt) { \
      PIN(t_[k][nt]); *(LAS bf16x8*)(lds + (KB) * 5120 + nt * 1024 + (L) * 16) = t_[k][nt]; } } while (0)
  f32x4 fin[4][2];
  unsigned req;

  unsigned tpace = (unsigned)__builtin_amdgcn_s_memrealtime();
#define WAITP(p, SMW) do { const int ex_ = (int)((0x1FE0u >> ((p) - 1)) & 1u) + (((SMW) && (p) <= 13) ? 1 : 0); \
    if (ex_ == 2) wait_vm<6>(); else if (ex_ == 1) wait_vm<5>(); else wait_vm<4>(); \
    if (G == 0) { unsigned t_; while ((int)((t_ = (unsigned)__builtin_amdgcn_s_memrealtime()) - tpace) < PACE) __builtin_amdgcn_s_sleep(1); tpace = t_; } } while (0)
  if constexpr (G == 0) {
    wait_vm<1>(); BAR_LGKM();
#pragma unroll
    for (int i = 0; i < 8; ++i) qf[i] = lds_rd128((lds_cptr)lds + (qi * 8 + i) * 1024 + lane * 16);
    BLOCK_MM(SLOT(0), false, true, 0, 0, 0, 0);
    wait_vm<0>(); BAR_LGKM();
    BLOCK_SM(SLOT(1), true, 1, 1, 0, 0);
#pragma unroll 1
    for (int p = 2; p <= 12; p += 2) {
      WAITP(p, true); BAR_LGKM();
      BLOCK_MM(SLOT(p), true, true, 1, p, 0, 0);
      WAITP(p + 1, false); BAR_LGKM();
      BLOCK_SM(SLOT(p + 1), false, 1, p + 1, 0, 0);
    }
    wait_vm<4>(); BAR_LGKM();
    { const int l_ = lane_id_opaque();
      const char* p = l_ < 8 ? a.ws + WS_OBUF + (b * NH + 8 * g + l_) * 32768 : l_ < 12 ? a.ws + WS_PBUF + ((b * 8 + r) * 4 + (l_ - 8)) * 40960 : a.out + (size_t)(b * SQ + 32 * g) * DM * 2;
      asm volatile("global_load_dword %0, %1, off sc1" : "+v"(wdummy) : "v"(p) : "memory"); }
    BLOCK_MM(SLOT(14), true, true, 2, 14, 128, 3);
    wait_vm<4>(); BAR_LGKM();
    BLOCK_SM(SLOT(15), false, 3, 15, 0, 0);
    BAR_LGKM();
    LATE_CONSTS;
    BLOCK_MM(0, true, false, 0, 0, 0, 0);
    *(LAS f32x4*)(lds + ST) = (f32x4){mref[0], row_sum(lrun[0]), mref[1], row_sum(lrun[1])};
#pragma unroll
    for (int i = 0; i < 8; ++i) *(LAS f32x4*)(lds + SC + i * 1024) = o[4 + (i >> 1)][i & 1];
    SBAR(); RELAY(1, qi, ln);
    BAR_LGKM();
    RELAY(1, 4 + qi, ln);
    BAR_LGKM();
    const f32x2 cx = *(const LAS f32x2*)(lds + ST);
    f32x4 t[8];
#pragma unroll
    for (int i = 0; i < 8; ++i) t[i] = *(const LAS f32x4*)(lds + SC + i * 1024);
    BAR_LGKM();
#pragma unroll
    for (int i = 0; i < 8; ++i)
#pragma unroll
      for (int e = 0; e < 4; ++e) fin[i >> 1][i & 1][e] = __builtin_fmaf(o[i >> 1][i & 1][e], cx[i & 1], t[i][e]);
  } else {
    BAR_LGKM();
#pragma unroll
    for (int i = 0; i < 8; ++i) qf[i] = lds_rd128((lds_cptr)lds + (qi * 8 + i) * 1024 + lane * 16);
#pragma unroll
    for (int j = 0; j < 8; ++j) issue_piece(2, SLOT(2), j);
    wait_vm<8>(); BAR_LGKM();
    BLOCK_MM(SLOT(1), false, true, 1, 1, 0, 0);
    wait_vm<4>(); BAR_LGKM();
    BLOCK_SM(SLOT(2), true, 1, 2, 0, 0);
#pragma unroll 1
    for (int p = 3; p <= 13; p += 2) {
      WAITP(p, true); BAR_LGKM();
      BLOCK_MM(SLOT(p), true, true, 1, p, 0, 0);
      WAITP(p + 1, false); BAR_LGKM();
      BLOCK_SM(SLOT(p + 1), false, 1, p + 1, 0, 0);
    }
    wait_vm<4>(); BAR_LGKM();
    BLOCK_MM(SLOT(15), true, true, 2, 15, 64, 4);
    wait_vm<4>(); BAR_LGKM();
    BLOCK_SM(SLOT(16), false, 2, 16, 80, 4);
    LATE_CONSTS;
    lrun[0] = row_sum(lrun[0]); lrun[1] = row_sum(lrun[1]);
    BAR_LGKM();
    const f32x4 st = *(const LAS f32x4*)(lds + ST);
    float cy[2], cx[2];
#pragma unroll
    for (int qt = 0; qt < 2; ++qt) {
      const float m = __builtin_fmaxf(mref[qt], st[2 * qt]);
      const float fy = EXP2(mref[qt] - m), fx = EXP2(st[2 * qt] - m);
      const float inv = 1.f / (fy * lrun[qt] + fx * st[2 * qt + 1]);
      cy[qt] = fy * inv; cx[qt] = fx * inv; }
    f32x4 t[8];
    SBAR();
#pragma unroll
    for (int i = 0; i < 16; ++i) {
      const int d = (i >> 3) * 4 + (i & 3), kk = (i >> 2) & 1;
      o[d][0] = MFMA16(fr[8 * kk + d], pf[kk][0], o[d][0]); o[d][1] = MFMA16(fr[8 * kk + d], pf[kk][1], o[d][1]); SBAR();
      if (i < 8) t[i] = *(const LAS f32x4*)(lds + SC + i * 1024);
      if (i >= 2 && i < 10) { t[i - 2] *= cx[i & 1]; PIN(t[i - 2]); }
      if (i == 10) *(LAS f32x2*)(lds + ST) = (f32x2){cx[0], cx[1]};
      if (i >= 10) {
#pragma unroll
        for (int j = (i - 10) * 8 / 6; j < (i - 9) * 8 / 6; ++j) *(LAS f32x4*)(lds + SC + j * 1024) = o[j >> 1][j & 1] * cy[j & 1];
      }
      SBAR();
    }
    BAR_LGKM();
#pragma unroll
    for (int i = 0; i < 8; ++i)
#pragma unroll
      for (int e = 0; e < 4; ++e) fin[i >> 1][i & 1][e] = __builtin_fmaf(o[4 + (i >> 1)][i & 1][e], cy[i & 1], t[i][e]);
  }
  const int lt = lane_id_opaque(), peer_t = PEER(lt);
  {
    const unsigned obase = WS_OBUF + bh * 32768 + (2 * qi * 4 + 2 * G) * 1024 + lt * 16;
#pragma unroll
    for (int i = 0; i < 4; ++i)
      __builtin_amdgcn_raw_buffer_store_b128(__builtin_bit_cast(u32x4, PK2(fin[2 * (i & 1)][i >> 1], fin[2 * (i & 1) + 1][i >> 1])), rs, obase + (i >> 1) * 4096 + (i & 1) * 1024, 0, AUX_SC1);
  }
  if constexpr (G == 1) BAR_LGKM();
  if constexpr (G == 0) {
#pragma unroll
    for (int i = 0; i < 13; ++i) *(LAS u32x4*)(lds + (i < 8 ? 96 + 8 * qi + i : 140 + 5 * qi + (i - 8)) * 1024 + lt * 16) = wl[i];
  }
  const unsigned inbox = WS_MBOX + bh * MBOXB + qi * 128 + (lt & 31) * 4;
  const unsigned fslot = WS_MBOX + peer_t * MBOXB + ((qi * 2 + G) * 16 + (lt < 8 ? r : 8 + g)) * 4;
  wait_vm<0>();
  PIN(wdummy);
  if (lt < 12) __builtin_amdgcn_raw_buffer_store_b32(req, rs, fslot, 0, AUX_SC1);
  BAR_LGKM();
  RELAY(3, 8 + 3 * wid + k, lt);
  BAR_LGKM();
  unsigned seen = 0;
#pragma unroll 1
  for (unsigned spins = 0; spins < SPIN_CAP; ++spins) {
    const unsigned f = __builtin_amdgcn_raw_buffer_load_b32(rs, inbox, 0, AUX_SC1);
    seen = (unsigned)__ballot(f == nonce) & 0x0FFF0FFFu;
    if ((seen & 0x00FF00FFu) == 0x00FF00FFu) break;
    __builtin_amdgcn_s_sleep(1);
  }
  asm volatile("" ::: "memory");
  u32x4 of[32];
  const unsigned ob = WS_OBUF + (b * NH + 8 * g) * 32768 + (2 * qi + G) * 4096 + lt * 16;
#define OLD(i) of[i] = __builtin_amdgcn_raw_buffer_load_b128(rs, ob + ((i) & 3) * 1024, ((r + ((i) >> 2)) & 7) * 32768, AUX_SC1)
#pragma unroll
  for (int i = 0; i < ODIST; ++i) OLD(i);
  unsigned req2 = __builtin_amdgcn_raw_buffer_load_b32(rs, WS_MBOX + peer_t * MBOXB + MB_REQ, 0, AUX_SC1);
  const unsigned pb = WS_PBUF + ((((b * 8 + r) * 4 + qi) * 4) * 2 + G) * 5120 + lt * 16;
  f32x4 res[5] = {};
#define ARD(AF, j, k, N0, NN) do { const lds_cptr pa_ = (lds_cptr)lds + ((r + (j)) & 7) * 20480 + lt * 16; \
    _Pragma("unroll") for (int nt = 0; nt < (NN); ++nt) AF[nt] = lds_rd128(pa_ + (k) * 5120 + ((N0) + nt) * 1024); } while (0)
#define OPASS(N0, NN) do { bf16x8 af[3][NN]; ARD(af[0], 0, 0, N0, NN); ARD(af[1], 0, 1, N0, NN); \
    _Pragma("unroll") for (int i = 0; i < 32; ++i) { SBAR(); \
      if ((N0) == 0 && i + ODIST < 32) OLD(i + ODIST); \
      if (i + 2 < 32) ARD(af[(i + 2) % 3], (i + 2) >> 2, (i + 2) & 3, N0, NN); \
      SBAR(); \
      _Pragma("unroll") for (int nt = 0; nt < (NN); ++nt) res[(N0) + nt] = MFMA16(af[i % 3][nt], __builtin_bit_cast(bf16x8, of[i]), res[(N0) + nt]); } \
    SBAR(); \
    if (qi != g) { _Pragma("unroll") for (int nt = (N0); nt < (N0) + (NN); ++nt) \
      __builtin_amdgcn_raw_buffer_store_b128(__builtin_bit_cast(u32x4, res[nt]), rs, pb + g * 10240 + nt * 1024, 0, AUX_SC1); } } while (0)
  OPASS(0, NT1);
  OPASS(NT1, 5 - NT1);
  const unsigned f2slot = WS_MBOX + peer_t * MBOXB + MB_F2 + (g * 2 + G) * 4;
  if (qi != g) {
    wait_vm<0>();
    if (lt == 8 + qi) __builtin_amdgcn_raw_buffer_store_b32(req2, rs, f2slot, 0, AUX_SC1);
  }
  if (seen != 0x0FFF0FFFu) {
#pragma unroll 1
    for (unsigned spins = 0; spins < SPIN_CAP; ++spins) {
      const unsigned f = __builtin_amdgcn_raw_buffer_load_b32(rs, inbox, 0, AUX_SC1);
      if ((((unsigned)__ballot(f == nonce)) & 0x0FFF0FFFu) == 0x0FFF0FFFu) break;
      __builtin_amdgcn_s_sleep(1);
    }
    asm volatile("" ::: "memory");
    const unsigned req3 = __builtin_amdgcn_raw_buffer_load_b32(rs, WS_MBOX + peer_t * MBOXB + MB_REQ, 0, AUX_SC1);
    if (qi != g && lt == 8 + qi && req3 != req2) __builtin_amdgcn_raw_buffer_store_b32(req3, rs, f2slot, 0, AUX_SC1);
    req2 = req3;
  }
  if (lt < 12 && req2 != req) __builtin_amdgcn_raw_buffer_store_b32(req2, rs, fslot, 0, AUX_SC1);
  if (qi == g) {
    const int src = lt & 3;
#pragma unroll 1
    for (unsigned spins = 0; spins < SPIN_CAP; ++spins) {
      const unsigned f = __builtin_amdgcn_raw_buffer_load_b32(rs, WS_MBOX + bh * MBOXB + MB_F2 + (src * 2 + G) * 4, 0, AUX_SC1);
      if (__all(f == nonce || src == g)) break;
      __builtin_amdgcn_s_sleep(1);
    }
    asm volatile("" ::: "memory");
    f32x4 pp[3][5];
#pragma unroll
    for (int s = 0; s < 3; ++s) { const int sg = s + (s >= g ? 1 : 0);
#pragma unroll
      for (int nt = 0; nt < 5; ++nt) pp[s][nt] = __builtin_bit_cast(f32x4, __builtin_amdgcn_raw_buffer_load_b128(rs, pb + sg * 10240 + nt * 1024, 0, AUX_SC1)); }
    char* orow = a.out + ((size_t)(b * SQ + 32 * qi + 16 * G + (lt & 15)) * DM + 80 * r + 4 * (lt >> 4)) * 2;
#pragma unroll
    for (int nt = 0; nt < 5; ++nt) {
      const f32x4 v = ((res[nt] + pp[0][nt]) + pp[1][nt]) + pp[2][nt];
      *(u32x2*)(orow + nt * 32) = (u32x2){cvtpk(v[0], v[1]), cvtpk(v[2], v[3])};
    }
  }
}

__global__ __launch_bounds__(512, 2) void mha_fused(const char* x, const char* kc, const char* vc, const char* wq, const char* wo, char* out, char* ws) {
  extern __shared__ __attribute__((aligned(16))) char lds_raw[];
  const unsigned lds0 = rfl((unsigned)(uintptr_t)lds_raw);
  const int wid = rfl(threadIdx.x >> 6);
  const Args a = {x, kc, vc, wq, wo, out, ws};
  if (wid == 7) {
    unsigned long long pc; unsigned len;
    asm volatile("s_getpc_b64 %0\n.Lmha_here:\n\ts_mov_b32 %1, .Lmha_end-.Lmha_here" : "=s"(pc), "=s"(len));
    unsigned off = ((blockIdx.x >> 3) * 16 + (threadIdx.x & 15)) * 128; off = off < len ? off : 0;
    asm volatile("s_mov_b32 m0, %2\n\ts_nop 0\n\tglobal_load_lds_dword %0, %1" :: "v"(off), "s"(pc), "s"(lds0 + 2 * PAIRB + SLOTB + 3072) : "memory");
  }
  if (wid < 4) body<0>(a, (lds_ptr)lds_raw, lds0, wid); else body<1>(a, (lds_ptr)lds_raw, lds0, wid);
  asm volatile(".Lmha_end:");
}

extern "C" void kernel_launch(void* const* d_in, const int* in_sizes, int n_in, void* d_out, int out_size, void* d_ws, size_t ws_size, hipStream_t stream) {
  (void)hipFuncSetAttribute((const void*)mha_fused, hipFuncAttributeMaxDynamicSharedMemorySize, LDS_BYTES);
  hipLaunchKernelGGL(mha_fused, dim3(256), dim3(512), LDS_BYTES, stream, (const char*)d_in[0], (const char*)d_in[1], (const char*)d_in[2],
                     (const char*)d_in[3], (const char*)d_in[4], (char*)d_out, (char*)d_ws);
}
```
